```python
import torch

def _softmax(x, axis=-1):
    return torch.softmax(x, dim=axis)
def _gelu(x):
    return torch.nn.functional.gelu(x, approximate="tanh")
def _silu(x):
    return torch.nn.functional.silu(x)
def _sigmoid(x):
    return torch.sigmoid(x)
def _layernorm(x, axis=-1, eps=1e-5):
    m = x.mean(dim=axis, keepdim=True)
    v = x.var(dim=axis, keepdim=True, unbiased=False)
    return (x - m) / torch.sqrt(v + eps)
def _scatter_add(tgt, idx, src, axis=0):
    return tgt.clone().index_add_(axis, idx, src)
def _segment_sum(src, seg, n):
    out = torch.zeros((n,) + src.shape[1:], dtype=src.dtype)
    return out.index_add_(0, seg, src)
def _pad(x, pw):
    flat = tuple(v for pair in reversed(pw) for v in pair)
    return torch.nn.functional.pad(x, flat)

kernel_name = 'gated_mix__mha_block_cached__compressed_kv_cla__decode'

def setup_inputs():

    _w = (lambda a: (lambda t: t.to(torch.bfloat16) if t.is_floating_point() else t)(torch.as_tensor(a)))
    def _A_setup():
        B, Sq, D = 2, 1, 512
        H, Sk, Dh = 32, 8192, 128
        return {
            "x": _w((lambda *s: torch.randn(*s, dtype=torch.bfloat16) * 0.05)(B, Sq, D)),
            "k_cache": _w((lambda *s: torch.randn(*s, dtype=torch.bfloat16) * 6.4)(B, H, Sk, Dh)),
            "v_cache": _w((lambda *s: torch.randn(*s, dtype=torch.bfloat16) * 3.2)(B, H, Sk, Dh)),
            "wq": _w((lambda *s: torch.randn(*s, dtype=torch.bfloat16) * 6.4)(D, H * Dh) * (D ** -0.5)),
            "wo": _w((lambda *s: torch.randn(*s, dtype=torch.bfloat16) * 3.2)(H * Dh, D) * ((H * Dh) ** -0.5)),
        }
    _A_in = _A_setup()

    def _B_setup():
        B, H, Sq, Sk, D, M = 2, 32, 1, 8192, 128, 512
        Dc = 4 * D
        return {
            "x": _w((lambda *s: torch.randn(*s, dtype=torch.bfloat16) * 0.05)(B, Sq, M)),
            "c_kv": _w((lambda *s: torch.randn(*s, dtype=torch.bfloat16) * 1.6)(B, Sk, Dc)),
            "w_q": _w((lambda *s: torch.randn(*s, dtype=torch.bfloat16) * 6.4)(M, H, D) * (M ** -0.5)),
            "w_uk": _w((lambda *s: torch.randn(*s, dtype=torch.bfloat16) * 6.4)(Dc, H, D) * (Dc ** -0.5)),
            "w_uv": _w((lambda *s: torch.randn(*s, dtype=torch.bfloat16) * 1.6)(Dc, H, D) * (Dc ** -0.5)),
            "w_o": _w((lambda *s: torch.randn(*s, dtype=torch.bfloat16) * 1.6)(H, D, M) * ((H * D) ** -0.5)),
        }
    _B_in = _B_setup()

    return {
        "A_x": _A_in["x"],
        "A_k_cache": _A_in["k_cache"],
        "A_v_cache": _A_in["v_cache"],
        "A_wq": _A_in["wq"],
        "A_wo": _A_in["wo"],
        "B_x": _B_in["x"],
        "B_c_kv": _B_in["c_kv"],
        "B_w_q": _B_in["w_q"],
        "B_w_uk": _B_in["w_uk"],
        "B_w_uv": _B_in["w_uv"],
        "B_w_o": _B_in["w_o"],
        "gate_w": _w((lambda *s: torch.randn(*s, dtype=torch.bfloat16) * 0.1)(512) * 16.0),
    }

def reference(A_x, A_k_cache, A_v_cache, A_wq, A_wo, B_x, B_c_kv, B_w_q, B_w_uk, B_w_uv, B_w_o, gate_w):
    def _A_ref(x, k_cache, v_cache, wq, wo):
        B, Sq, D = x.shape
        H, Dh = 32, 128
        q = (lambda x, *d: x.permute(*d))((x @ wq).reshape(B, Sq, H, Dh), 0, 2, 1, 3)
        scores = torch.einsum("bhqd,bhkd->bhqk", q, k_cache) * (Dh ** -0.5)
        p = _softmax(scores, axis=-1)
        o = torch.einsum("bhqk,bhkd->bhqd", p, v_cache)
        return (lambda x, *d: x.permute(*d))(o, 0, 2, 1, 3).reshape(B, Sq, H * Dh) @ wo

    def _B_ref(x, c_kv, w_q, w_uk, w_uv, w_o):
        B, Sq, M = x.shape[0], x.shape[1], x.shape[2]
        Sk = c_kv.shape[1]
        H, D = w_q.shape[1], w_q.shape[2]
        q = torch.einsum("bqm,mhd->bhqd", x, w_q)
        k = torch.einsum("bkc,chd->bhkd", c_kv, w_uk)
        v = torch.einsum("bkc,chd->bhkd", c_kv, w_uv)
        scores = torch.einsum("bhqd,bhkd->bhqk", q, k) * (D ** -0.5)
        off = Sk - Sq
        qi = torch.arange(Sq)[:, None]
        kj = torch.arange(Sk)[None, :]
        cmask = kj <= (qi + off)
        scores = torch.where(cmask[None, None], scores, -1e9)
        p = _softmax(scores, axis=-1)
        ctx = torch.einsum("bhqk,bhkd->bhqd", p, v)
        return torch.einsum("bhqd,hdm->bqm", ctx, w_o)

    x = A_x
    g = _sigmoid((x * gate_w).sum(-1, keepdims=True))
    a = _A_ref(x, A_k_cache, A_v_cache, A_wq, A_wo)
    b = _B_ref(x, B_c_kv, B_w_q, B_w_uk, B_w_uv, B_w_o)
    return g * a + (1 - g) * b
```

```cpp
#include <hip/hip_runtime.h>
#include <hip/hip_bf16.h>
#include <stdint.h>

#define GAS __attribute__((address_space(1)))
#define LAS __attribute__((address_space(3)))
#define DI __device__ __forceinline__
typedef unsigned v4u __attribute__((ext_vector_type(4)));
typedef unsigned v2u __attribute__((ext_vector_type(2)));
typedef float f32x4 __attribute__((ext_vector_type(4)));
typedef float f32x2 __attribute__((ext_vector_type(2)));
typedef __bf16 bf16x8 __attribute__((ext_vector_type(8)));
typedef __bf16 bf16x2 __attribute__((ext_vector_type(2)));
typedef short s16x4 __attribute__((ext_vector_type(4)));
typedef unsigned long long u64;
typedef __amdgpu_buffer_rsrc_t rsrc_t;

namespace {
constexpr int NWG = 256, NTHR = 512;
constexpr int AUX_SC1 = 16, AUX_NT = 2;
constexpr int AUX_K = AUX_NT, AUX_V = AUX_NT, AUX_W = 0, AUX_C = 0;
constexpr int WB = 8, WIN0 = 4, WIN = 5;
constexpr unsigned OOB = 0x7F000000u;
constexpr float C2 = 0.08838834764831845f * 1.4426950408889634f;
constexpr u64 EPOCH_STEP = 0x9E3779B97F4A7C15ull;
constexpr unsigned SPIN_CAP = 1u << 16;

constexpr int WS_EPOCH = 0, WS_F1 = 256, WS_F2 = 512;
constexpr int WS_QABS = 4096;
constexpr int WS_PART = WS_QABS + 65536;
constexpr int WS_PST = WS_PART + 1048576;
constexpr int WS_Y = WS_PST + 8192;
constexpr int WS_SA = WS_Y + 1048576;
constexpr int WS_QA = WS_SA + 8192;
constexpr int WS_QB = WS_QA + 131072;
constexpr int WS_END = WS_QB + 65536;

constexpr int ROWB = 1056;
constexpr int L_RING = 0, TILEB = 64 * ROWB;
constexpr int L_RED = 2 * TILEB;
constexpr int L_OSL = L_RED + 8192, OSLB = 288;
constexpr int L_STAT = L_OSL + 16 * OSLB;
constexpr int L_QST = L_STAT + 128;
constexpr int L_OLAT = L_QST + 2048, OLATB = 1056;
constexpr int L_FLAG = L_OLAT + 8192;
constexpr int L_END = L_FLAG + 64;
static_assert(L_END <= 163840, "LDS");

struct Params {
  const void *x, *kc, *vc, *wq, *wo, *ckv, *w_q, *w_uk, *w_uv, *w_o, *gate;
  void* out;
  void* ws;
};

DI rsrc_t mk_rsrc(const void* p, unsigned bytes) { return __builtin_amdgcn_make_buffer_rsrc((void*)p, (short)0, (int)bytes, 0x00020000); }
template <int AUX> DI v4u bld(rsrc_t r, unsigned voff, unsigned soff) { return __builtin_amdgcn_raw_buffer_load_b128(r, (int)voff, (int)soff, AUX); }
DI float dot2(unsigned a, unsigned b, float c) { return __builtin_amdgcn_fdot2_f32_bf16(__builtin_bit_cast(bf16x2, a), __builtin_bit_cast(bf16x2, b), c, false); }
DI unsigned pk_bf16(float lo, float hi) { bf16x2 r = {(__bf16)lo, (__bf16)hi}; return __builtin_bit_cast(unsigned, r); }
DI float bf_lo(unsigned u) { return __builtin_bit_cast(float, u << 16); }
DI float bf_hi(unsigned u) { return __builtin_bit_cast(float, u & 0xffff0000u); }
template <int CTRL> DI float dpp(float x) { return __builtin_bit_cast(float, __builtin_amdgcn_mov_dpp(__builtin_bit_cast(int, x), CTRL, 0xf, 0xf, true)); }
constexpr int XOR1 = 0xB1, XOR2 = 0x4E, XOR7 = 0x141, ROR8 = 0x128, ROR4 = 0x124, BCAST = 0x150;
DI float fold32(float a, float b) {
  auto s = __builtin_amdgcn_permlane32_swap(__float_as_uint(a), __float_as_uint(b), false, false);
  return __uint_as_float(s[0]) + __uint_as_float(s[1]);
}
DI float fold16(float a, float b) {
  auto s = __builtin_amdgcn_permlane16_swap(__float_as_uint(a), __float_as_uint(b), false, false);
  return __uint_as_float(s[0]) + __uint_as_float(s[1]);
}
DI float xrow_max(float x) {
  auto s = __builtin_amdgcn_permlane16_swap(__float_as_uint(x), __float_as_uint(x), false, false);
  x = fmaxf(__uint_as_float(s[0]), __uint_as_float(s[1]));
  auto t = __builtin_amdgcn_permlane32_swap(__float_as_uint(x), __float_as_uint(x), false, false);
  return fmaxf(__uint_as_float(t[0]), __uint_as_float(t[1]));
}
DI float xrow_sum(float x) { x = fold16(x, x); return fold32(x, x); }
DI f32x4 mfma(v4u a, v4u b, f32x4 c) { return __builtin_amdgcn_mfma_f32_16x16x32_bf16(__builtin_bit_cast(bf16x8, a), __builtin_bit_cast(bf16x8, b), c, 0, 0, 0); }
DI v2u tr_read(LAS char* p) { return __builtin_bit_cast(v2u, __builtin_amdgcn_ds_read_tr16_b64_v4i16((LAS s16x4*)p)); }
DI float exp2_(float x) { return __builtin_amdgcn_exp2f(x); }
#define SB() __builtin_amdgcn_sched_barrier(0)
#define DRAIN() asm volatile("s_waitcnt vmcnt(0)" ::: "memory")
template <int N> DI void vm_wait() { asm volatile("s_waitcnt vmcnt(%0)" :: "i"(N) : "memory"); }
#define BAR() do { asm volatile("s_waitcnt lgkmcnt(0)" ::: "memory"); __builtin_amdgcn_s_barrier(); asm volatile("" ::: "memory"); } while (0)
template <int AUX> DI void glds(rsrc_t r, unsigned voff, unsigned soff, LAS char* dst) {
  unsigned keep, lds = (unsigned)(uintptr_t)dst;
  soff = __builtin_amdgcn_readfirstlane(soff); lds = __builtin_amdgcn_readfirstlane(lds);
  if constexpr (AUX == 2)
    asm volatile("s_mov_b32 %0, m0\n\ts_mov_b32 m0, %1\n\ts_nop 3\n\tbuffer_load_dwordx4 %2, %3, %4 offen nt lds\n\ts_mov_b32 m0, %0"
                 : "=&s"(keep) : "s"(lds), "v"(voff), "s"(r), "s"(soff) : "memory");
  else
    asm volatile("s_mov_b32 %0, m0\n\ts_mov_b32 m0, %1\n\ts_nop 3\n\tbuffer_load_dwordx4 %2, %3, %4 offen lds\n\ts_mov_b32 m0, %0"
                 : "=&s"(keep) : "s"(lds), "v"(voff), "s"(r), "s"(soff) : "memory");
}
DI u64 ld_tag(const GAS u64* p) { return __hip_atomic_load(p, __ATOMIC_RELAXED, __HIP_MEMORY_SCOPE_AGENT); }
DI u64 call_tag(const void* ws) {
  u64 e;
  SB();
  asm volatile("s_load_dwordx2 %0, %1, 0x0\n\ts_waitcnt lgkmcnt(0)" : "=s"(e) : "s"(ws) : "memory");
  SB();
  return e + EPOCH_STEP;
}
DI void st_tag(GAS u64* p, u64 v) { __hip_atomic_store(p, v, __ATOMIC_RELAXED, __HIP_MEMORY_SCOPE_AGENT); }

template <int N, int I0 = 0, int I1 = N> DI void proj_issue(rsrc_t rw, int h, int row0, int lane, v4u (&W)[N]) {
  const unsigned voff = (unsigned)(lane >> 4) * (N * 8192u) + (unsigned)(lane & 15) * 16u;
#pragma unroll
  for (int i = I0; i < I1; ++i) { W[i] = bld<AUX_W>(rw, voff, (unsigned)(row0 + i) * 8192u + (unsigned)h * 256u); SB(); }
}
template <int N> DI void x_issue(const Params& p, int row0, int lane, v4u (&xr)[2][N / 8]) {
  const rsrc_t rx = mk_rsrc(p.x, 2048);
#pragma unroll
  for (int i = 0; i < N / 4; ++i) xr[i / (N / 8)][i % (N / 8)] = bld<0>(rx, (unsigned)(lane >> 4) * (2u * N) + 16u * (i % (N / 8)), (unsigned)(i / (N / 8)) * 1024u + (unsigned)row0 * 2u);
  SB();
}
template <int N> DI void proj_partial(const v4u (&W)[N], const v4u (&xr)[2][N / 8], LAS char* L, int w, int lane) {
  float a[2][8];
#pragma unroll
  for (int k = 0; k < 8; ++k) a[0][k] = a[1][k] = 0.f;
#pragma unroll
  for (int i = 0; i < N / 2; ++i) {
#pragma unroll
    for (int k = 0; k < 4; ++k) {
      const unsigned lo = __builtin_amdgcn_perm(W[2 * i + 1][k], W[2 * i][k], 0x05040100u), hi = __builtin_amdgcn_perm(W[2 * i + 1][k], W[2 * i][k], 0x07060302u);
#pragma unroll
      for (int v = 0; v < 2; ++v) { a[v][2 * k] = dot2(lo, xr[v][i >> 2][i & 3], a[v][2 * k]); a[v][2 * k + 1] = dot2(hi, xr[v][i >> 2][i & 3], a[v][2 * k + 1]); }
    }
  }
  float u[8]; f32x4 t;
#pragma unroll
  for (int k = 0; k < 8; ++k) u[k] = fold32(a[0][k], a[1][k]);
#pragma unroll
  for (int k = 0; k < 4; ++k) t[k] = fold16(u[k], u[k + 4]);
  const int r = lane >> 4, c = lane & 15;
  *(LAS f32x4*)(L + L_RED + w * 1024 + (r >> 1) * 512 + c * 32 + (r & 1) * 16) = t;
}
DI float proj_sum(LAS char* L, int tid) {
  float q = 0.f;
#pragma unroll
  for (int ww = 0; ww < 8; ++ww) q += *(LAS float*)(L + L_RED + ww * 1024 + tid * 4);
  return q;
}
template <int AUX> DI void oproj_issue(LAS char* L, rsrc_t rw, int h, int w, int lane, int i) {
  const unsigned sw = 2u * (((unsigned)(lane >> 4) & 1u) | ((unsigned)(i & 1) << 1));
  const unsigned voff = (unsigned)(lane >> 3) * 1024u + ((((unsigned)lane & 7u) ^ sw) << 4);
  glds<AUX>(rw, voff, (unsigned)(h * 128 + 8 * i) * 1024u + (unsigned)w * 128u, L + L_RING + w * 16384 + i * 1024);
}
DI void oproj_frags(LAS char* L, int w, int lane, v4u (&F)[4][4]) {
  const int g = lane >> 4, q = (lane & 15) >> 2, p = lane & 3;
#pragma unroll
  for (int ct = 0; ct < 4; ++ct)
#pragma unroll
    for (int s = 0; s < 4; ++s)
#pragma unroll
      for (int t = 0; t < 2; ++t) {
        const int rowl = 8 * g + 4 * t + q;
        const int sw = 2 * (((rowl >> 1) & 1) | (((rowl >> 3) & 1) << 1));
        const v2u x = tr_read(L + L_RING + w * 16384 + (32 * s + rowl) * 128 + (((2 * ct + (p >> 1)) ^ sw) << 4) + 8 * (p & 1));
        F[ct][s][2 * t] = x[0]; F[ct][s][2 * t + 1] = x[1];
      }
}
DI float pair_max(float x) {
  auto s = __builtin_amdgcn_permlane16_swap(__float_as_uint(x), __float_as_uint(x), false, false);
  return fmaxf(__uint_as_float(s[0]), __uint_as_float(s[1]));
}
DI f32x2 oproj_finish(LAS char* L, int lane, const v4u (&F)[4][4], float& M, float& Ls) {
  const int n = lane & 15, g = lane >> 4;
  v4u o[4];
#pragma unroll
  for (int s = 0; s < 4; ++s) o[s] = *(LAS v4u*)(L + L_OSL + n * OSLB + (4 * s + g) * 16);
  const f32x4 s0 = *(LAS f32x4*)(L + L_STAT + g * 32), s1 = *(LAS f32x4*)(L + L_STAT + g * 32 + 16);
  f32x4 d[4];
#pragma unroll
  for (int ct = 0; ct < 4; ++ct) d[ct] = f32x4{0.f, 0.f, 0.f, 0.f};
#pragma unroll
  for (int s = 0; s < 4; ++s)
#pragma unroll
    for (int ct = 0; ct < 4; ++ct) d[ct] = mfma(o[s], F[ct][s], d[ct]);
  M = pair_max(fmaxf(fmaxf(s0[0], s0[2]), fmaxf(s1[0], s1[2])));
  const float e0 = exp2_(s0[0] - M), e1 = exp2_(s0[2] - M), e2 = exp2_(s1[0] - M), e3 = exp2_(s1[2] - M);
  Ls = fmaf(e0, s0[1], fmaf(e1, s0[3], fmaf(e2, s1[1], e3 * s1[3])));
  Ls = fold16(Ls, Ls);
  float y[4];
#pragma unroll
  for (int ct = 0; ct < 4; ++ct) y[ct] = fmaf(e0, d[ct][0], fmaf(e1, d[ct][1], fmaf(e2, d[ct][2], e3 * d[ct][3])));
  return f32x2{fold16(y[0], y[1]), fold16(y[2], y[3])};
}
DI u64 gran(u64 tag, float v) { return (tag & ~0xffffffull) | (u64)(__float_as_uint(v) >> 8); }
DI float gran_val(u64 g) { return __uint_as_float((unsigned)g << 8); }
DI bool gran_ok(u64 g, u64 tag) { return ((g ^ tag) >> 24) == 0; }
DI void gran_store(rsrc_t rws, unsigned off, u64 tag, float v) {
  const u64 g = gran(tag, v);
  __builtin_amdgcn_raw_buffer_store_b64(v2u{(unsigned)g, (unsigned)(g >> 32)}, rws, (int)off, 0, AUX_SC1);
}
DI unsigned pinned(unsigned x) { asm volatile("" : "+v"(x)); return x; }
DI void osl_clear(LAS char* L, int w, int lane) {
  const unsigned row = (unsigned)(8 * (lane >> 5) + w);
  *(LAS v2u*)(L + L_OSL + row * OSLB + (lane & 31) * 8) = v2u{0u, 0u};
  if ((lane & 31) == 0) *(LAS f32x2*)(L + L_STAT + row * 8) = f32x2{-INFINITY, 0.f};
}

template <int NK> DI bool wait_tags(const GAS u64* f, unsigned mask, u64 tag, int lane) {
  for (unsigned spin = 0;; ++spin) {
    bool ok = true;
#pragma unroll
    for (int k = 0; k < NK; ++k) { const u64 v = ld_tag(f + 64 * k + lane); ok &= (v == tag) || !((mask >> k) & 1u); }
    if (__all(ok)) return true;
    if (spin >= SPIN_CAP) return false;
    __builtin_amdgcn_s_sleep(16);
  }
}
template <int NK> DI bool edge_wait(LAS char* L, const GAS u64* f, unsigned mask, u64 tag, int w, int lane) {
  if (w == 0) {
    const bool ok = wait_tags<NK>(f, mask, tag, lane);
    __builtin_amdgcn_fence(__ATOMIC_ACQUIRE, "wavefront");
    if (lane == 0) *(volatile LAS unsigned*)(L + L_FLAG) = ok ? 1u : 0u;
  }
  BAR();
  return __builtin_amdgcn_readfirstlane(*(volatile LAS unsigned*)(L + L_FLAG)) != 0u;
}

struct AState { float m, l; f32x2 acc[4]; };
DI void a_flush(LAS char* L, const AState& s, int b, int w, int lane) {
  const int r = lane >> 4, c = lane & 15;
  float u[4];
#pragma unroll
  for (int k = 0; k < 4; ++k) u[k] = fold32(s.acc[k >> 1][k & 1], s.acc[2 + (k >> 1)][k & 1]);
  const float t0 = fold16(u[0], u[2]), t1 = fold16(u[1], u[3]);
  *(LAS unsigned*)(L + L_OSL + (8 * b + w) * OSLB + c * 16 + r * 4) = pk_bf16(t0, t1);
  if (lane == 0) *(LAS f32x2*)(L + L_STAT + (8 * b + w) * 8) = f32x2{s.m, s.l};
}
template <bool REFILL, int THR = 24, bool LAST = false, int NS = 4> DI void a_tile(AState& s, v4u (&K)[4], v4u (&V)[4], const v4u& q, rsrc_t rk, rsrc_t rv, unsigned voff, unsigned nxt, int c, int live = 4) {
  float x[4] = {0.f, 0.f, 0.f, 0.f};
#pragma unroll
  for (int i = 0; i < NS; ++i) {
    float d = dot2(K[i][0], q[0], 0.f); d = dot2(K[i][1], q[1], d); d = dot2(K[i][2], q[2], d); x[i] = dot2(K[i][3], q[3], d);
    SB(); if (REFILL) { if (THR < 24) vm_wait<THR - 1>(); K[i] = bld<AUX_K>(rk, voff + 1024u * i, nxt); } SB();
  }
  const float a0 = x[0] + dpp<ROR8>(x[0]), a2 = x[2] + dpp<ROR8>(x[2]), a1 = x[1] + dpp<ROR8>(x[1]), a3 = x[3] + dpp<ROR8>(x[3]);
  const float y0 = (c & 8) ? a2 : a0, y1 = (c & 8) ? a3 : a1;
  const float b0 = y0 + dpp<XOR7>(y0), b1 = y1 + dpp<XOR7>(y1);
  float z = (c & 4) ? b1 : b0;
  z += dpp<XOR2>(z); z += dpp<XOR1>(z);
  z *= C2;
  if (NS < 4) z = (c >> 2) < live ? z : -INFINITY;
  asm volatile("" : "+v"(z));
  float pm = fmaxf(z, dpp<ROR4>(z)); pm = fmaxf(pm, dpp<ROR8>(pm));
  const float mn = LAST ? xrow_max(fmaxf(s.m, pm)) : fmaxf(s.m, pm);
  const float al = exp2_(s.m - mn), p = exp2_(z - mn);
  s.m = mn; s.l = fmaf(s.l, al, p);
  if (LAST) { s.l += dpp<ROR4>(s.l); s.l += dpp<ROR8>(s.l); s.l = xrow_sum(s.l); }
#pragma unroll
  for (int k = 0; k < 4; ++k) s.acc[k] *= al;
#pragma unroll
  for (int i = 0; i < NS; ++i) {
    const float pi = i == 0 ? dpp<BCAST + 0>(p) : i == 1 ? dpp<BCAST + 4>(p) : i == 2 ? dpp<BCAST + 8>(p) : dpp<BCAST + 12>(p);
#pragma unroll
    for (int k = 0; k < 4; ++k) s.acc[k] = __builtin_elementwise_fma(f32x2{pi, pi}, f32x2{bf_lo(V[i][k]), bf_hi(V[i][k])}, s.acc[k]);
    asm volatile("" : "+v"(s.acc[0]), "+v"(s.acc[1]), "+v"(s.acc[2]), "+v"(s.acc[3]));
    SB(); if (REFILL) { if (THR < 24) vm_wait<THR - 1>(); V[i] = bld<AUX_V>(rv, voff + 1024u * i, nxt); } SB();
  }
}

DI void role_a(const Params& p, LAS char* L, int w, int lane, int b, int h, int j) {
  const int lo = 171 * j, hi = j == 2 ? 512 : lo + 171;
  const rsrc_t rk = mk_rsrc(p.kc, 1u << 27), rv = mk_rsrc(p.vc, 1u << 27), rwo = mk_rsrc(p.wo, 1u << 22);
  const unsigned voff = (unsigned)lane * 16u, base = (unsigned)(b * 32 + h) << 21;
  auto toff = [&](int i) -> unsigned { return base + ((unsigned)(lo + w + 8 * i) << 12); };
  v4u K[4][4], V[4][4];
  const GAS u64* gq = (const GAS u64*)((GAS char*)p.ws + WS_QA) + (b * 32 + h) * 256 + lane;
  u64 tag = 0, v[4] = {0, 0, 0, 0};
#pragma unroll
  for (int u = 0; u < 3; ++u) {
    const unsigned o = toff(u);
    if (u == 1) tag = call_tag(p.ws);
#pragma unroll
    for (int i = 0; i < 4; ++i) { if (8 * u + i >= WB) vm_wait<WIN0 - 1>(); K[u][i] = bld<AUX_K>(rk, voff + 1024u * i, o); SB(); }
#pragma unroll
    for (int i = 0; i < 4; ++i) { if (8 * u + 4 + i >= WB) vm_wait<WIN0 - 1>(); V[u][i] = bld<AUX_V>(rv, voff + 1024u * i, o); SB(); }
  }
  if (w == 0) {
#pragma unroll
    for (int i = 0; i < 4; ++i) v[i] = ld_tag(gq + 64 * i);
    SB();
  }
  const int live = w + 8 < 4 * (hi - lo - 168) ? 2 : 1;
#pragma unroll
  for (int i = 0; i < 2; ++i) {
    const unsigned o = i < live ? base + ((unsigned)(lo + 168) << 12) + (unsigned)(w + 8 * i) * 1024u : OOB;
    vm_wait<WIN0 - 1>();
    K[3][i] = bld<AUX_K>(rk, voff, o); SB();
    vm_wait<WIN0 - 1>();
    V[3][i] = bld<AUX_V>(rv, voff, o); SB();
  }
  osl_clear(L, w, lane);
  if (w == 0) {
    bool ok;
    for (unsigned spin = 0;; ++spin) {
      ok = __all((((v[0] ^ tag) | (v[1] ^ tag) | (v[2] ^ tag) | (v[3] ^ tag)) >> 24) == 0);
      if (ok || spin >= SPIN_CAP) break;
      if (spin) __builtin_amdgcn_s_sleep(2);
#pragma unroll
      for (int i = 0; i < 4; ++i) v[i] = ld_tag(gq + 64 * i);
    }
#pragma unroll
    for (int i = 0; i < 2; ++i) *(LAS __bf16*)(L + L_QST + i * 128 + lane * 2) = (__bf16)(__uint_as_float((unsigned)v[i] << 8) + __uint_as_float((unsigned)v[2 + i] << 8));
    if (lane == 0) *(volatile LAS unsigned*)(L + L_FLAG) = ok ? 1u : 0u;
  }
  BAR();
  if (__builtin_amdgcn_readfirstlane(*(volatile LAS unsigned*)(L + L_FLAG)) == 0u) return;
  const int c = lane & 15;
  const v4u q = *(LAS v4u*)(L + L_QST + c * 16);
  AState s; s.m = -INFINITY; s.l = 0.f;
#pragma unroll
  for (int k = 0; k < 4; ++k) s.acc[k] = f32x2{0.f, 0.f};
  a_tile<true>(s, K[0], V[0], q, rk, rv, voff, toff(3), c);
  a_tile<false, 24, false, 2>(s, K[3], V[3], q, rk, rv, voff, OOB, c, live);
  a_tile<true, WIN>(s, K[1], V[1], q, rk, rv, voff, toff(4), c);
  a_tile<false>(s, K[2], V[2], q, rk, rv, voff, OOB, c);
  oproj_issue<AUX_W>(L, rwo, h, w, lane, 0);
  a_tile<true, WIN>(s, K[0], V[0], q, rk, rv, voff, toff(5), c);
  oproj_issue<AUX_W>(L, rwo, h, w, lane, 1);
  a_tile<false>(s, K[1], V[1], q, rk, rv, voff, OOB, c);
  for (int it = 0; it < 14; ++it) {
    oproj_issue<AUX_W>(L, rwo, h, w, lane, 2 + it);
    a_tile<true, WIN>(s, K[0], V[0], q, rk, rv, voff, toff(6 + it), c);
    __builtin_amdgcn_s_barrier();
  }
  vm_wait<8>();
  v4u F[4][4];
  oproj_frags(L, w, lane, F);
  const bool mine = (lane >> 5) == b;
  const unsigned yo = pinned(mine ? (unsigned)(WS_Y + (((b * 32 + h) * 4 + j) * 512 + 64 * w + (lane & 31)) * 8) : OOB);
  const unsigned so = pinned(mine && w == 0 && (lane & 31) < 3 ? (unsigned)(WS_SA + ((b * 32 + h) * 16 + 3 * j + (lane & 31)) * 8) : OOB);
  SB();
  a_tile<true, WIN>(s, K[0], V[0], q, rk, rv, voff, toff(20), c);
  a_tile<false, 24, true>(s, K[0], V[0], q, rk, rv, voff, OOB, c);
  a_flush(L, s, b, w, lane);
  BAR();
  float M, Ls;
  const f32x2 y = oproj_finish(L, lane, F, M, Ls);
  const rsrc_t rws = mk_rsrc(p.ws, WS_END);
  gran_store(rws, so, tag, (lane & 2) ? Ls : (lane & 1) ? __uint_as_float(__float_as_uint(M) << 8) : M);
  gran_store(rws, yo, tag, y[0]);
  gran_store(rws, yo + 256u, tag, y[1]);
}

DI void role_head(const Params& p, LAS char* L, int w, int lane, int h) {
  const int r = lane >> 4, c = lane & 15, tid = w * 64 + lane;
  const rsrc_t rws = mk_rsrc(p.ws, WS_END);
  GAS char* ws = (GAS char*)p.ws;
  u64 tag;
  {
    v4u xa[2][1], xb[2][2], Wa[8], Wb[16];
    x_issue<8>(p, 256 + 32 * w, lane, xa);
    proj_issue<8>(mk_rsrc(p.wq, 1u << 22), h, 256 + 32 * w, lane, Wa);
    x_issue<16>(p, 64 * w, lane, xb);
    __builtin_amdgcn_s_barrier();
    proj_issue<16, 0, 4>(mk_rsrc(p.w_q, 1u << 22), h, 64 * w, lane, Wb);
    tag = call_tag(p.ws);
    osl_clear(L, w, lane);
    proj_partial<8>(Wa, xa, L, w, lane);
    BAR();
    if (tid < 256) st_tag((GAS u64*)(ws + WS_QA) + ((tid >> 7) * 32 + h) * 256 + 128 + (tid & 127), gran(tag, proj_sum(L, tid)));
    proj_issue<16, 4, 16>(mk_rsrc(p.w_q, 1u << 22), h, 64 * w, lane, Wb);
    BAR();
    proj_partial<16>(Wb, xb, L, w, lane);
    BAR();
    if (tid < 256) st_tag((GAS u64*)(ws + WS_QB) + h * 256 + tid, (tag & ~0xffffull) | (u64)(pk_bf16(proj_sum(L, tid), 0.f) & 0xffffu));
  }
  v4u U[16], F[4][4];
  {
    const rsrc_t ru = mk_rsrc(p.w_uv, 1u << 22), rwo = mk_rsrc(p.w_o, 1u << 22);
#pragma unroll
    for (int i = 0; i < 16; ++i) {
      const unsigned voff = (unsigned)r * 8192u + (((unsigned)c ^ (2u * (unsigned)(4 * (i & 1) + r))) << 4);
      glds<AUX_W>(ru, voff, (unsigned)(64 * w + 4 * i) * 8192u + (unsigned)h * 256u, L + L_RING + (64 * w + 4 * i) * 256);
    }
    DRAIN();
    BAR();
    const int q = c >> 2, pp = c & 3;
    LAS char* a0 = L + L_RING + (4 * r + q) * 256 + (((2 * w + (pp >> 1)) ^ (2 * (4 * (r & 1) + q))) << 4) + 8 * (pp & 1);
#pragma unroll
    for (int s = 0; s < 16; ++s) {
      const v2u x0 = tr_read(a0 + s * 8192), x1 = tr_read(a0 + s * 8192 + 4096);
      U[s] = v4u{x0[0], x0[1], x1[0], x1[1]};
    }
#pragma unroll
    for (int s = 0; s < 16; ++s) asm volatile("" : "+v"(U[s]));
    BAR();
#pragma unroll
    for (int i = 0; i < 16; ++i) oproj_issue<AUX_W>(L, rwo, h, w, lane, i);
    if (tid < 66) *(LAS v4u*)(L + L_OLAT + 2 * OLATB + tid * 16) = v4u{0u, 0u, 0u, 0u};
    DRAIN();
    oproj_frags(L, w, lane, F);
#pragma unroll
    for (int i = 0; i < 16; ++i) asm volatile("" : "+v"(F[i >> 2][i & 3]));
  }
  if (!edge_wait<1>(L, (const GAS u64*)(ws + WS_F2), lane < 32 ? 1u : 0u, tag, w, lane)) return;
  {
    const int b = w >> 2;
    v4u st[2], pv[4];
#pragma unroll
    for (int k = 0; k < 2; ++k) st[k] = bld<AUX_SC1>(rws, (unsigned)r * 32u + 16u * k, (unsigned)(WS_PST + (b * 32 + h) * 128));
#pragma unroll
    for (int k = 0; k < 4; ++k) pv[k] = bld<AUX_SC1>(rws, (unsigned)r * (4u * 32768u) + (unsigned)c * 16u, (unsigned)(WS_PART + ((b * 16 + k) * 32 + h) * 1024 + (w & 3) * 256));
    const float mm[4] = {__uint_as_float(st[0][0]), __uint_as_float(st[0][2]), __uint_as_float(st[1][0]), __uint_as_float(st[1][2])};
    const float ll[4] = {__uint_as_float(st[0][1]), __uint_as_float(st[0][3]), __uint_as_float(st[1][1]), __uint_as_float(st[1][3])};
    const float M = xrow_max(fmaxf(fmaxf(mm[0], mm[1]), fmaxf(mm[2], mm[3])));
    float e[4], Ls = 0.f;
#pragma unroll
    for (int k = 0; k < 4; ++k) { e[k] = exp2_(mm[k] - M); Ls = fmaf(e[k], ll[k], Ls); }
    const float inv = 1.f / xrow_sum(Ls);
    f32x2 o[4];
#pragma unroll
    for (int i = 0; i < 4; ++i) {
      o[i] = f32x2{0.f, 0.f};
#pragma unroll
      for (int k = 0; k < 4; ++k) o[i] = __builtin_elementwise_fma(f32x2{e[k], e[k]}, f32x2{bf_lo(pv[k][i]), bf_hi(pv[k][i])}, o[i]);
    }
    float u[4];
#pragma unroll
    for (int k = 0; k < 4; ++k) u[k] = fold32(o[k >> 1][k & 1], o[2 + (k >> 1)][k & 1]);
    const float t0 = fold16(u[0], u[2]), t1 = fold16(u[1], u[3]);
    *(LAS unsigned*)(L + L_OLAT + b * OLATB + (w & 3) * 256 + c * 16 + r * 4) = pk_bf16(t0 * inv, t1 * inv);
  }
  BAR();
  {
    v4u ob[16];
#pragma unroll
    for (int s = 0; s < 16; ++s) ob[s] = *(LAS v4u*)(L + L_OLAT + (c < 2 ? c : 2) * OLATB + s * 64 + r * 16);
    f32x4 d0 = {0.f, 0.f, 0.f, 0.f}, d1 = d0;
#pragma unroll
    for (int s = 0; s < 16; s += 2) { d0 = mfma(U[s], ob[s], d0); d1 = mfma(U[s + 1], ob[s + 1], d1); }
    d0 += d1;
    if (c < 2) {
      *(LAS v2u*)(L + L_OSL + 8 * c * OSLB + (16 * w + 4 * r) * 2) = v2u{pk_bf16(d0[0], d0[1]), pk_bf16(d0[2], d0[3])};
      if (w == 0 && r == 0) *(LAS f32x2*)(L + L_STAT + 8 * c * 8) = f32x2{0.f, 1.f};
    }
  }
  BAR();
  {
    float M2, L2;
    const f32x2 y = oproj_finish(L, lane, F, M2, L2);
    const unsigned yo = (unsigned)(WS_Y + ((((lane >> 5) * 32 + h) * 4 + 3) * 512 + 64 * w + (lane & 31)) * 8);
    gran_store(rws, yo, tag, y[0]);
    gran_store(rws, yo + 256u, tag, y[1]);
  }
  if (h == 0 && w == 0) {
    const GAS u64* g = (const GAS u64*)(ws + WS_SA) + lane * 16;
    bool ok = false;
    for (unsigned spin = 0; spin < SPIN_CAP && !ok; ++spin) {
      ok = __all(gran_ok(ld_tag(g), tag) && gran_ok(ld_tag(g + 3), tag) && gran_ok(ld_tag(g + 6), tag));
      __builtin_amdgcn_s_sleep(1);
    }
    if (ok && lane == 0) st_tag((GAS u64*)(ws + WS_EPOCH), tag);
  }
}

DI void role_latent(const Params& p, LAS char* L, int w, int lane, int j) {
  const int bb = j >> 4, r = lane >> 4, c = lane & 15, tid = w * 64 + lane;
  const rsrc_t rws = mk_rsrc(p.ws, WS_END), rc = mk_rsrc(p.ckv, 1u << 24);
  GAS char* ws = (GAS char*)p.ws;
  u64 tag;
  {
    v4u xa[2][1], Wa[8], U[16];
    x_issue<8>(p, 32 * w, lane, xa);
    proj_issue<8>(mk_rsrc(p.wq, 1u << 22), j, 32 * w, lane, Wa);
    __builtin_amdgcn_s_barrier();
    proj_issue<16, 0, 4>(mk_rsrc(p.w_uk, 1u << 22), j, 64 * w, lane, U);
    tag = call_tag(p.ws);
    proj_partial<8>(Wa, xa, L, w, lane);
    BAR();
    if (tid < 256) st_tag((GAS u64*)(ws + WS_QA) + ((tid >> 7) * 32 + j) * 256 + (tid & 127), gran(tag, proj_sum(L, tid)));
    proj_issue<16, 4, 16>(mk_rsrc(p.w_uk, 1u << 22), j, 64 * w, lane, U);
    if (w == 7) {
      const GAS u64* g = (const GAS u64*)(ws + WS_QB) + j * 256 + lane;
      bool ok;
      u64 v[4];
      for (unsigned spin = 0;; ++spin) {
#pragma unroll
        for (int i = 0; i < 4; ++i) v[i] = ld_tag(g + 64 * i);
        ok = __all((((v[0] ^ tag) | (v[1] ^ tag) | (v[2] ^ tag) | (v[3] ^ tag)) >> 16) == 0);
        if (ok || spin >= SPIN_CAP) break;
        __builtin_amdgcn_s_sleep(2);
      }
#pragma unroll
      for (int i = 0; i < 4; ++i) *(LAS unsigned short*)(L + L_OSL + i * 128 + lane * 2) = (unsigned short)v[i];
      if (lane == 0) *(volatile LAS unsigned*)(L + L_FLAG) = ok ? 1u : 0u;
    }
    BAR();
    if (__builtin_amdgcn_readfirstlane(*(volatile LAS unsigned*)(L + L_FLAG)) == 0u) return;
#pragma unroll
    for (int b = 0; b < 2; ++b) {
      const v4u q = *(LAS v4u*)(L + L_OSL + b * 256 + c * 16);
      float x[16];
#pragma unroll
      for (int i = 0; i < 16; ++i) { float d = dot2(U[i][0], q[0], 0.f); d = dot2(U[i][1], q[1], d); d = dot2(U[i][2], q[2], d); x[i] = dot2(U[i][3], q[3], d); }
#define TR_STEP(HALF, CTRL, BIT) _Pragma("unroll") for (int k = 0; k < (HALF); ++k) { const float lo_ = x[k] + dpp<CTRL>(x[k]), hi_ = x[k + (HALF)] + dpp<CTRL>(x[k + (HALF)]); x[k] = (c & (BIT)) ? hi_ : lo_; }
      TR_STEP(8, ROR8, 8) TR_STEP(4, XOR7, 4) TR_STEP(2, XOR2, 2) TR_STEP(1, XOR1, 1)
#undef TR_STEP
      *(LAS __bf16*)(L + L_QST + b * 1024 + tid * 2) = (__bf16)x[0];
    }
    BAR();
    if (tid < 128) {
      const v4u v = *(LAS v4u*)(L + L_QST + tid * 16);
      __builtin_amdgcn_raw_buffer_store_b128(v, rws, WS_QABS + ((((tid >> 6) * 2 + (j >> 4)) * 16 + ((tid & 63) >> 2)) * 64 + (tid & 3) * 16 + (j & 15)) * 16, 0, AUX_SC1);
    }
    DRAIN();
    BAR();
    if (tid == 0) st_tag((GAS u64*)(ws + WS_F1) + j, tag);
  }
  const unsigned kbase = (unsigned)(bb * 8192 + (j & 15) * 512) * 1024u;
  auto c_issue = [&](int t, int i0, int i1) {
#pragma unroll
    for (int i = i0; i < i1; ++i) glds<AUX_C>(rc, (unsigned)lane * 16u, kbase + (unsigned)(64 * t + 8 * w + i) * 1024u, L + L_RING + (t & 1) * TILEB + (8 * w + i) * ROWB);
  };
  c_issue(0, 0, 8);
  if (!edge_wait<1>(L, (const GAS u64*)(ws + WS_F1), lane < 32 ? 1u : 0u, tag, w, lane)) return;
  f32x4 acc[4][2];
  f32x4 ls[2] = {{0.f, 0.f, 0.f, 0.f}, {0.f, 0.f, 0.f, 0.f}};
  float m[2] = {-INFINITY, -INFINITY};
  {
    const int kq = w & 3, hh = w >> 2;
    v4u Q[16];
    {
      v4u qs[4];
#pragma unroll
      for (int i = 0; i < 4; ++i) qs[i] = bld<AUX_SC1>(rws, (unsigned)lane * 16u, (unsigned)(WS_QABS + bb * 32768 + (4 * w + i) * 1024));
#pragma unroll
      for (int i = 0; i < 4; ++i) *(LAS v4u*)(L + L_RING + TILEB + (4 * w + i) * 1024 + lane * 16) = qs[i];
      BAR();
#pragma unroll
      for (int s = 0; s < 16; ++s) Q[s] = *(LAS v4u*)(L + L_RING + TILEB + (16 * hh + s) * 1024 + lane * 16);
#pragma unroll
      for (int s = 0; s < 16; ++s) asm volatile("" : "+v"(Q[s]));
      BAR();
      c_issue(1, 0, 6);
    }
#pragma unroll
    for (int ct = 0; ct < 4; ++ct) acc[ct][0] = acc[ct][1] = f32x4{0.f, 0.f, 0.f, 0.f};
    for (int t = 0; t < 8; ++t) {
      if (t < 7) vm_wait<6>(); else DRAIN();
      BAR();
      LAS char* T = L + L_RING + (t & 1) * TILEB;
      v4u Cf[16];
#pragma unroll
      for (int s = 0; s < 16; ++s) Cf[s] = *(LAS v4u*)(T + (16 * kq + c) * ROWB + (4 * s + r) * 16);
      SB();
      f32x4 sc = {0.f, 0.f, 0.f, 0.f};
#pragma unroll
      for (int s = 0; s < 16; ++s) sc = mfma(Cf[s], Q[s], sc);
      *(LAS f32x4*)(L + L_RED + tid * 16) = sc * C2;
      BAR();
      if (t + 1 < 8) c_issue(t + 1, 6, 8);
      v4u P[2][2];
#pragma unroll
      for (int u = 0; u < 2; ++u) {
        f32x4 sv[4];
#pragma unroll
        for (int k = 0; k < 4; ++k) sv[k] = *(LAS f32x4*)(L + L_RED + ((u * 4 + k) * 64 + lane) * 16);
        float pm = fmaxf(fmaxf(sv[0][0], sv[0][1]), fmaxf(sv[0][2], sv[0][3]));
#pragma unroll
        for (int k = 1; k < 4; ++k) pm = fmaxf(fmaxf(fmaxf(pm, sv[k][0]), sv[k][1]), fmaxf(sv[k][2], sv[k][3]));
        pm = xrow_max(pm);
        const float mn = fmaxf(m[u], pm), al = exp2_(m[u] - mn);
#pragma unroll
        for (int k = 0; k < 4; ++k) {
          float e[4];
#pragma unroll
          for (int i = 0; i < 4; ++i) e[i] = exp2_(sv[k][i] - mn);
          P[u][k >> 1][2 * (k & 1)] = pk_bf16(e[0], e[1]); P[u][k >> 1][2 * (k & 1) + 1] = pk_bf16(e[2], e[3]);
        }
        m[u] = mn;
#pragma unroll
        for (int ct = 0; ct < 4; ++ct) acc[ct][u] *= al;
        const v4u ones = {0x3f803f80u, 0x3f803f80u, 0x3f803f80u, 0x3f803f80u};
        ls[u] = mfma(ones, P[u][1], mfma(ones, P[u][0], ls[u] * al));
      }
#pragma unroll
      for (int ct = 0; ct < 4; ++ct)
#pragma unroll
        for (int ks = 0; ks < 2; ++ks) {
          LAS char* a0 = T + (32 * ks + 4 * r + (c >> 2)) * ROWB + (64 * w + 16 * ct + 4 * (c & 3)) * 2;
          const v2u x0 = tr_read(a0), x1 = tr_read(a0 + 16 * ROWB);
          const v4u A = {x0[0], x0[1], x1[0], x1[1]};
          acc[ct][0] = mfma(A, P[0][ks], acc[ct][0]);
          acc[ct][1] = mfma(A, P[1][ks], acc[ct][1]);
        }
      BAR();
      if (t + 2 < 8) c_issue(t + 2, 0, 6);
    }
  }
#pragma unroll
  for (int u = 0; u < 2; ++u) {
#pragma unroll
    for (int e = 0; e < 2; ++e) {
      const f32x4 a0 = acc[2 * e][u], a1 = acc[2 * e + 1][u];
      __builtin_amdgcn_raw_buffer_store_b128(v4u{pk_bf16(a0[0], a0[1]), pk_bf16(a0[2], a0[3]), pk_bf16(a1[0], a1[1]), pk_bf16(a1[2], a1[3])}, rws,
                                             WS_PART + (j * 32 + 16 * u + c) * 1024 + (2 * w + e) * 64 + r * 16, 0, AUX_SC1);
    }
    if (w == 0 && r == 0) __builtin_amdgcn_raw_buffer_store_b64(v2u{__float_as_uint(m[u]), __float_as_uint(ls[u][0])}, rws, WS_PST + ((bb * 32 + 16 * u + c) * 16 + (j & 15)) * 8, 0, AUX_SC1);
  }
  DRAIN();
  BAR();
  if (tid == 0) st_tag((GAS u64*)(ws + WS_F2) + j, tag);
  float gt;
  {
    const v4u xv = *(const GAS v4u*)((const GAS char*)p.x + bb * 1024 + lane * 16), gv = *(const GAS v4u*)((const GAS char*)p.gate + lane * 16);
    float d = dot2(xv[0], gv[0], 0.f); d = dot2(xv[1], gv[1], d); d = dot2(xv[2], gv[2], d); d = dot2(xv[3], gv[3], d);
    d += dpp<XOR1>(d); d += dpp<XOR2>(d); d += dpp<XOR7>(d); d += dpp<ROR8>(d); d = xrow_sum(d);
    gt = 1.f / (1.f + __expf(-d));
  }
  const GAS u64* gy = (const GAS u64*)(ws + WS_Y) + ((bb * 32 + 4 * w + r) * 4 + 2 * (c >> 3)) * 512 + 32 * (j & 15) + (c & 7);
  const GAS u64* gs = (const GAS u64*)(ws + WS_SA) + (bb * 32 + 4 * w + r) * 16 + (c < 9 ? c : 0);
  {
    u64 v[9];
    auto sweep = [&](u64 (&x)[9]) {
#pragma unroll
      for (int i = 0; i < 8; ++i) x[i] = ld_tag(gy + (i >> 2) * 512 + 8 * (i & 3));
      x[8] = ld_tag(gs);
      SB();
    };
    auto complete = [&](const u64 (&x)[9]) {
      u64 d = 0;
#pragma unroll
      for (int i = 0; i < 9; ++i) d |= x[i] ^ tag;
      return __all((d >> 24) == 0) != 0;
    };
    unsigned spin = 0;
    for (;; ++spin) {
      sweep(v);
      if (complete(v) || spin >= SPIN_CAP) break;
      __builtin_amdgcn_s_sleep(1);
    }
    if (spin >= SPIN_CAP && lane == 0) *(volatile LAS unsigned*)(L + L_FLAG) = 0u;
    const float sv = gran_val(v[8]);
    const float sm = __uint_as_float(__float_as_uint(sv) | (__float_as_uint(dpp<0x101>(sv)) >> 8));
    const float m0 = dpp<BCAST + 0>(sm), l0 = dpp<BCAST + 2>(sv), m1 = dpp<BCAST + 3>(sm), l1 = dpp<BCAST + 5>(sv), m2 = dpp<BCAST + 6>(sm), l2 = dpp<BCAST + 8>(sv);
    const float M = fmaxf(fmaxf(m0, m1), m2);
    const float e0 = exp2_(m0 - M), e1 = exp2_(m1 - M), e2 = exp2_(m2 - M);
    const float sc = gt / fmaf(e0, l0, fmaf(e1, l1, e2 * l2));
    const float w0 = ((c & 8) ? e2 : e0) * sc, w1 = (c & 8) ? 1.f - gt : e1 * sc;
    f32x4 o;
#pragma unroll
    for (int i = 0; i < 4; ++i) { o[i] = fmaf(gran_val(v[i]), w0, gran_val(v[4 + i]) * w1); o[i] += dpp<ROR8>(o[i]); }
    const float u0 = fold32(o[0], o[2]), u1 = fold32(o[1], o[3]), z = fold16(u0, u1);
    if (c < 8) *(LAS float*)(L + L_OLAT + w * 128 + (8 * r + c) * 4) = z;
    BAR();
    if (tid < 16) {
      f32x2 a = {0.f, 0.f};
#pragma unroll
      for (int ww = 0; ww < 8; ++ww) a += *(LAS f32x2*)(L + L_OLAT + ww * 128 + tid * 8);
      const bool good = *(volatile LAS unsigned*)(L + L_FLAG) != 0u;
      __builtin_amdgcn_raw_buffer_store_b32(pk_bf16(a[0], a[1]), mk_rsrc(p.out, 2048), good ? (bb * 512 + 32 * (j & 15)) * 2 + tid * 4 : (int)OOB, 0, AUX_SC1);
    }
  }
}

__global__ void __launch_bounds__(NTHR, 2) gated_mix_decode(Params p) {
  __shared__ __attribute__((aligned(16))) char smem[L_END];
  LAS char* L = (LAS char*)smem;
  const int lane = threadIdx.x & 63, w = __builtin_amdgcn_readfirstlane(threadIdx.x >> 6);
  const int id = blockIdx.x;
  if (id < 32) role_latent(p, L, w, lane, id);
  else if (id < 64) role_head(p, L, w, lane, id - 32);
  else {
    const int a = id - 64, k = a >> 3, h = (a & 7) * 4 + k / 6, b = (k % 6) / 3, j = k % 3;
    role_a(p, L, w, lane, b, h, j);
  }
}
}

extern "C" void kernel_launch(void* const* d_in, const int* in_sizes, int n_in, void* d_out, int out_size, void* d_ws, size_t ws_size, hipStream_t stream) {
  Params p;
  p.x = d_in[0]; p.kc = d_in[1]; p.vc = d_in[2]; p.wq = d_in[3]; p.wo = d_in[4];
  p.ckv = d_in[6]; p.w_q = d_in[7]; p.w_uk = d_in[8]; p.w_uv = d_in[9]; p.w_o = d_in[10]; p.gate = d_in[11];
  p.out = d_out; p.ws = d_ws;
  gated_mix_decode<<<NWG, NTHR, 0, stream>>>(p);
}
```
